```python
import torch

def _softmax(x, axis=-1):
    return torch.softmax(x, dim=axis)
def _gelu(x):
    return torch.nn.functional.gelu(x, approximate="tanh")
def _silu(x):
    return torch.nn.functional.silu(x)
def _sigmoid(x):
    return torch.sigmoid(x)
def _layernorm(x, axis=-1, eps=1e-5):
    m = x.mean(dim=axis, keepdim=True)
    v = x.var(dim=axis, keepdim=True, unbiased=False)
    return (x - m) / torch.sqrt(v + eps)
def _scatter_add(tgt, idx, src, axis=0):
    return tgt.clone().index_add_(axis, idx, src)
def _segment_sum(src, seg, n):
    out = torch.zeros((n,) + src.shape[1:], dtype=src.dtype)
    return out.index_add_(0, seg, src)
def _pad(x, pw):
    flat = tuple(v for pair in reversed(pw) for v in pair)
    return torch.nn.functional.pad(x, flat)

kernel_name = 'gated_mix__ssm_conv_h3__dense_activation_gelu__prompt'

def setup_inputs():

    _w = (lambda a: (lambda t: t.to(torch.bfloat16) if t.is_floating_point() else t)(torch.as_tensor(a)))
    def _A_setup():
        B, S, D = 1, 2048, 640
        ks = 4
        return {
            "q": _w((lambda *s: torch.randn(*s, dtype=torch.bfloat16) * 0.05)(B, S, D)),
            "k": _w((lambda *s: torch.randn(*s, dtype=torch.bfloat16) * 0.2)(B, S, D)),
            "v": _w((lambda *s: torch.randn(*s, dtype=torch.bfloat16) * 0.2)(B, S, D)),
            "shift_k": _w((lambda *s: torch.randn(*s, dtype=torch.bfloat16) * 0.2)(D, ks) * 0.5),
            "ssm_k": _w((lambda *s: torch.randn(*s, dtype=torch.bfloat16) * 0.8)(D, S) * (1.0 / S ** 0.5)),
            "ssm_D": _w((lambda *s: torch.randn(*s, dtype=torch.bfloat16) * 0.2)(D)),
        }
    _A_in = _A_setup()

    def _B_setup():
        B, S, D, F = 1, 2048, 640, 1536
        return {
            "x": _w((lambda *s: torch.randn(*s, dtype=torch.bfloat16) * 0.05)(B, S, D)),
            "w1": _w((lambda *s: torch.randn(*s, dtype=torch.bfloat16) * 3.2)(D, F) * (D ** -0.5)),
            "b1": _w((lambda *s: torch.randn(*s, dtype=torch.bfloat16) * 3.2)(F) * 0.01),
            "w2": _w((lambda *s: torch.randn(*s, dtype=torch.bfloat16) * 0.05)(F, D) * (F ** -0.5)),
            "b2": _w((lambda *s: torch.randn(*s, dtype=torch.bfloat16) * 0.1)(D) * 0.01),
        }
    _B_in = _B_setup()

    return {
        "A_q": _A_in["q"],
        "A_k": _A_in["k"],
        "A_v": _A_in["v"],
        "A_shift_k": _A_in["shift_k"],
        "A_ssm_k": _A_in["ssm_k"],
        "A_ssm_D": _A_in["ssm_D"],
        "B_x": _B_in["x"],
        "B_w1": _B_in["w1"],
        "B_b1": _B_in["b1"],
        "B_w2": _B_in["w2"],
        "B_b2": _B_in["b2"],
        "gate_w": _w((lambda *s: torch.randn(*s, dtype=torch.bfloat16) * 0.05)(640) * 16.0),
    }

def reference(A_q, A_k, A_v, A_shift_k, A_ssm_k, A_ssm_D, B_x, B_w1, B_b1, B_w2, B_b2, gate_w):
    def _A_ref(q, k, v, shift_k, ssm_k, ssm_D):
        B, S, D = q.shape
        ks = shift_k.shape[-1]
        k_pad = _pad(k, ((0, 0), (ks - 1, 0), (0, 0)))
        k_shift = torch.zeros_like(k)
        for t in range(ks):
            k_shift = k_shift + k_pad[:, t:t + S, :] * shift_k[:, ks - 1 - t][None, None, :]
        kv = k_shift * v
        L = 2 * S
        kv_pad = _pad(kv, ((0, 0), (0, S), (0, 0)))
        kf_pad = _pad(ssm_k, ((0, 0), (0, S)))
        kv_f = (lambda x, axes=(-2,-1): torch.fft.rfft2(x.float(), dim=axes))(kv_pad[:, None, :, :], axes=(1, 2))
        kf_f = (lambda x, axes=(-2,-1): torch.fft.rfft2(x.float(), dim=axes))(kf_pad[:, None, :], axes=(1, 2))
        kf_f = (lambda x, *d: x.permute(*d))(kf_f, 1, 2, 0)
        y_f = kv_f * kf_f[None, :, :, :]
        y = (lambda x, s, axes=(-2,-1): torch.fft.irfft2(x, s=s, dim=axes).to(torch.bfloat16))(y_f, (1, L), axes=(1, 2))[:, 0, :S, :]
        y = y + kv * ssm_D[None, None, :]
        return q * y

    def _B_ref(x, w1, b1, w2, b2):
        h = torch.einsum("bsd,df->bsf", x, w1) + b1
        h = _gelu(h)
        return torch.einsum("bsf,fd->bsd", h, w2) + b2

    x = A_q
    g = _sigmoid((x * gate_w).sum(-1, keepdims=True))
    a = _A_ref(x, A_k, A_v, A_shift_k, A_ssm_k, A_ssm_D)
    b = _B_ref(x, B_w1, B_b1, B_w2, B_b2)
    return g * a + (1 - g) * b
```

```cpp
#include <hip/hip_runtime.h>
#include <hip/hip_bf16.h>
#include <stdint.h>

namespace gm {
typedef unsigned short u16;
typedef unsigned long long u64;
typedef short bf16x8 __attribute__((ext_vector_type(8)));
typedef short v4s __attribute__((ext_vector_type(4)));
typedef float f32x4 __attribute__((ext_vector_type(4)));
typedef float f32x2 __attribute__((ext_vector_type(2)));
typedef unsigned v4u __attribute__((ext_vector_type(4)));
typedef unsigned v2u __attribute__((ext_vector_type(2)));
typedef __bf16 bf2 __attribute__((ext_vector_type(2)));
#define LAS __attribute__((address_space(3)))
#define GAS __attribute__((address_space(1)))
#define CAS __attribute__((address_space(4)))
#define NOPK
#define DEVI __device__ __forceinline__ NOPK

constexpr int S = 2048, D = 640, F = 1536;
constexpr int WS_EP = 0;
constexpr int WS_FA = 4096;
constexpr int FGS = 4352;
constexpr int WS_FY = 8192;
constexpr int WS_FH = WS_FY + 8 * FGS;
constexpr int WS_DBG = 262144;
constexpr int WS_KVT = 1 << 20;
constexpr int WS_YT = WS_KVT + D * 4096;
constexpr int WS_W2T = WS_YT + D * 4096;
constexpr int WS_HT = WS_W2T + 8 * 48 * 5 * 1024;
constexpr int WS_G = WS_HT + 128 * 48 * 1024;
constexpr int WS_END = WS_G + S * 4;
constexpr int STG = 28672;
constexpr int L_GW = 3 * STG;
constexpr int L_SK = L_GW + 2048;
constexpr int L_B1 = L_SK + 1024;
constexpr int HCP = 4640, UCP = 4224, HCH = 4 * HCP, UCH = 2 * UCP;
constexpr int L_R0 = 0;
constexpr int L_U = 32784;
constexpr int L_R1 = L_U + 3 * UCH;
constexpr int L_H = L_R1 + 49152;
constexpr int L_B2 = L_H + 3 * HCH;
constexpr int L_CNT = L_B2 + 160;
constexpr int L_KT = L_H;
constexpr int L_VT = L_KT + 17408;
constexpr int L_KVO = L_VT + 16384;
constexpr int L_W2B = L_H;
constexpr int L_XCH = L_H;
constexpr int L_YI = L_XCH + 40960;
constexpr int LDS_BYTES = 163840;
static_assert(L_B1 + 1024 <= L_H, "lds");
static_assert(L_KVO + 16384 <= L_B2, "lds");
static_assert(L_YI + 10240 <= L_B2, "lds");
static_assert(L_CNT + 64 <= LDS_BYTES, "lds");
static_assert(L_H % 16 == 0 && L_R1 % 16 == 0 && L_B2 % 16 == 0 && L_CNT % 16 == 0, "lds");
constexpr int C_H = 0, C_Y = 1, C_OKH = 2, C_OKY = 3, C_STG = 4, C_CONV = 5, C_CONVC = 6, C_RING = 8;

struct Params {
  const u16 *q, *k, *v, *sk, *ssmk, *ssmD, *w1, *b1, *w2, *b2, *gw;
  u16* out;
  unsigned char* ws;
};

DEVI f32x4 mfma(v4u a, v4u b, f32x4 c) {
  return __builtin_amdgcn_mfma_f32_16x16x32_bf16(__builtin_bit_cast(bf16x8, a), __builtin_bit_cast(bf16x8, b), c, 0, 0, 0);
}
DEVI float u2f(unsigned x) { return __builtin_bit_cast(float, x); }
DEVI unsigned f2u(float x) { return __builtin_bit_cast(unsigned, x); }
DEVI bool allof(bool p) { return __builtin_amdgcn_ballot_w64(!p) == 0ull; }
DEVI float blo(unsigned x) { return u2f(x << 16); }
DEVI float bhi(unsigned x) { return u2f(x & 0xffff0000u); }
DEVI unsigned pk(float a, float b) { f32x2 f = {a, b}; return __builtin_bit_cast(unsigned, __builtin_convertvector(f, bf2)); }
DEVI float rbf(float a) { return blo(pk(a, 0.f)); }
DEVI v2u trd(LAS unsigned char* p) { return __builtin_bit_cast(v2u, __builtin_amdgcn_ds_read_tr16_b64_v4i16((LAS v4s*)p)); }
DEVI float dot2(unsigned a, unsigned b, float c) { return __builtin_amdgcn_fdot2_f32_bf16(__builtin_bit_cast(bf2, a), __builtin_bit_cast(bf2, b), c, false); }
DEVI v4u cat(v2u a, v2u b) { return v4u{a.x, a.y, b.x, b.y}; }
DEVI v4u srd(const void* p, unsigned bytes) {
  const u64 a = (u64)p;
  return v4u{(unsigned)a, (unsigned)(a >> 32) & 0xffffu, bytes, 0x00020000u};
}
DEVI u64 rfl64(u64 x) {
  return ((u64)__builtin_amdgcn_readfirstlane((unsigned)(x >> 32)) << 32) | (unsigned)__builtin_amdgcn_readfirstlane((unsigned)x);
}
#define RFL(x) __builtin_amdgcn_readfirstlane(x)
#define WAIT_V(n) asm volatile("s_waitcnt vmcnt(%0)" ::"i"(n) : "memory")
#define WAIT_L0() asm volatile("s_waitcnt lgkmcnt(0)" ::: "memory")
#define BAR() do { WAIT_L0(); __builtin_amdgcn_s_barrier(); asm volatile("" ::: "memory"); } while (0)
#define SCHED() __builtin_amdgcn_sched_barrier(0)
#define PRIO(n) __builtin_amdgcn_s_setprio(n)
#define PIN(x) asm volatile("" : "+v"(x))
#define GLDS_(POL, rs, lds, voff, soff) \
  asm volatile("s_mov_b32 m0, %0\n\ts_nop 0\n\tbuffer_load_dwordx4 %1, %2, %3 offen " POL "lds" ::"s"(lds), "v"(voff), "s"(rs), "s"(soff) : "memory")
#define GLDS48_(POL, rs, lds, voff, soff) \
  asm volatile("s_mov_b32 m0, %0\n\ts_mov_b32 exec_hi, 0xffff\n\tbuffer_load_dwordx4 %1, %2, %3 offen " POL "lds\n\ts_mov_b32 exec_hi, -1" ::"s"(lds), "v"(voff), "s"(rs), "s"(soff) : "memory")
#define GLDS(rs, lds, voff, soff) GLDS_("", rs, lds, voff, soff)
#define GLDS_NT(rs, lds, voff, soff) GLDS_("nt ", rs, lds, voff, soff)
#define LD128(dst, rs, voff, soff) asm volatile("buffer_load_dwordx4 %0, %1, %2, %3 offen sc1" : "=v"(dst) : "v"(voff), "s"(rs), "s"(soff) : "memory")
#define LD128NT(dst, rs, voff, soff) asm volatile("buffer_load_dwordx4 %0, %1, %2, %3 offen nt" : "=v"(dst) : "v"(voff), "s"(rs), "s"(soff) : "memory")
#define LD128P(dst, rs, voff, soff) asm volatile("buffer_load_dwordx4 %0, %1, %2, %3 offen" : "=v"(dst) : "v"(voff), "s"(rs), "s"(soff) : "memory")
#define LD64(dst, rs, voff, soff) asm volatile("buffer_load_dwordx2 %0, %1, %2, %3 offen sc1" : "=v"(dst) : "v"(voff), "s"(rs), "s"(soff) : "memory")
#define LD64P(dst, rs, voff, soff) asm volatile("buffer_load_dwordx2 %0, %1, %2, %3 offen" : "=v"(dst) : "v"(voff), "s"(rs), "s"(soff) : "memory")
#define LD16P(dst, rs, voff, soff) asm volatile("buffer_load_ushort %0, %1, %2, %3 offen" : "=v"(dst) : "v"(voff), "s"(rs), "s"(soff) : "memory")
#define ST128(val, rs, voff, soff) asm volatile("buffer_store_dwordx4 %0, %1, %2, %3 offen sc1\n\ts_nop 1" ::"v"(val), "v"(voff), "s"(rs), "s"(soff) : "memory")
#define ST64(val, rs, voff, soff) asm volatile("buffer_store_dwordx2 %0, %1, %2, %3 offen sc1" ::"v"(val), "v"(voff), "s"(rs), "s"(soff) : "memory")
#define ST32(val, rs, voff, soff) asm volatile("buffer_store_dword %0, %1, %2, %3 offen sc1" ::"v"(val), "v"(voff), "s"(rs), "s"(soff) : "memory")
#define OPOL ""
#define STO64(val, rs, voff, soff) asm volatile("buffer_store_dwordx2 %0, %1, %2, %3 offen" OPOL ::"v"(val), "v"(voff), "s"(rs), "s"(soff) : "memory")
#define STO16(val, rs, voff, soff) asm volatile("buffer_store_short %0, %1, %2, %3 offen" OPOL ::"v"(val), "v"(voff), "s"(rs), "s"(soff) : "memory")

#define GM_SLEEP 4
#define STAMP(i)

DEVI bool eq2(v4u x, u64 tag) { return x.x == (unsigned)tag && x.y == (unsigned)(tag >> 32) && x.z == (unsigned)tag && x.w == (unsigned)(tag >> 32); }
DEVI bool eq1(v2u x, u64 tag) { return x.x == (unsigned)tag && x.y == (unsigned)(tag >> 32); }
DEVI void spin1(v4u sws, int voff, u64 tag) {
#pragma nounroll
  for (int spin = 0; spin < (1 << 16); ++spin) {
    v2u x;
    LD64(x, sws, voff, 0);
    WAIT_V(0); PIN(x);
    if (allof(eq1(x, tag))) break;
    __builtin_amdgcn_s_sleep(GM_SLEEP);
  }
}
DEVI void spin4(v4u sws, int voff, u64 tag) {
#pragma nounroll
  for (int spin = 0; spin < (1 << 16); ++spin) {
    v4u x, y;
    LD128(x, sws, voff, 0);
    LD128(y, sws, voff, 1024);
    WAIT_V(0); PIN(x); PIN(y);
    if (allof(eq2(x, tag) && eq2(y, tag))) break;
    __builtin_amdgcn_s_sleep(GM_SLEEP);
  }
}

#define GM_STGM 4
#define GM_HPUB 4
DEVI void gelu8(f32x4 a, f32x4 b, unsigned& o0, unsigned& o1, unsigned& o2, unsigned& o3) {
  f32x2 x0 = {a[0], a[1]}, x1 = {a[2], a[3]}, x2 = {b[0], b[1]}, x3 = {b[2], b[3]}, t0, t1, t2, t3;
  const f32x2 c1 = {-2.3022081981f, -2.3022081981f}, c2 = {-0.1029432397f, -0.1029432397f};
  t0 = x0 * (c1 + c2 * x0 * x0); t1 = x1 * (c1 + c2 * x1 * x1); t2 = x2 * (c1 + c2 * x2 * x2); t3 = x3 * (c1 + c2 * x3 * x3);
#define EX2(t) t = f32x2{__builtin_amdgcn_exp2f(t.x), __builtin_amdgcn_exp2f(t.y)} + f32x2{1.f, 1.f}
#define RC2(t, x_) t = x_ * f32x2{__builtin_amdgcn_rcpf(t.x), __builtin_amdgcn_rcpf(t.y)}
  EX2(t0); EX2(t1); EX2(t2); EX2(t3);
  RC2(t0, x0); RC2(t1, x1); RC2(t2, x2); RC2(t3, x3);
  o0 = pk(t0.x, t0.y); o1 = pk(t1.x, t1.y); o2 = pk(t2.x, t2.y); o3 = pk(t3.x, t3.y);
}
DEVI float adds(float a, float b) { float r; asm("v_add_f32 %0, %1, %2" : "=v"(r) : "v"(a), "v"(b)); return r; }
DEVI float fmas(float a, float b, float c) { float r; asm("v_fma_f32 %0, %1, %2, %3" : "=v"(r) : "v"(a), "s"(b), "v"(c)); return r; }
DEVI f32x4 add4s(f32x4 a, f32x4 b) { return f32x4{adds(a[0], b[0]), adds(a[1], b[1]), adds(a[2], b[2]), adds(a[3], b[3])}; }

template <int PART> struct HalfB;
template <> struct HalfB<0> { static constexpr int b[4] = {7, 6, 1, 0}; static constexpr int mx = 7; };
template <> struct HalfB<1> { static constexpr int b[4] = {5, 4, 3, 2}; static constexpr int mx = 5; };

DEVI void lds_arrive(LAS unsigned char* L, int word, int lane) {
  WAIT_L0();
  if (lane == 0) __hip_atomic_fetch_add((LAS unsigned*)(L + L_CNT) + word, 1u, __ATOMIC_RELAXED, __HIP_MEMORY_SCOPE_WORKGROUP);
}
DEVI void lds_wait(LAS unsigned char* L, int word, unsigned want) {
  volatile LAS unsigned* c = (volatile LAS unsigned*)(L + L_CNT) + word;
#pragma nounroll
  for (int spin = 0; spin < (1 << 22); ++spin) { if ((unsigned)RFL(*c) >= want) break; __builtin_amdgcn_s_sleep(1); }
  asm volatile("" ::: "memory");
}

#define SARG
#define SPASS

template <int NY, class Mid>
DEVI void gemm2(const Params& P, LAS unsigned char* L, v4u (&wf)[30], v4u sws, v4u sq, u64 tag, int tid, int lane, int w, int p, int rt, int cg, Mid mid SARG) {
  const int tw = w & 3, fh = w >> 2, l15 = lane & 15, q = lane >> 4, l16 = lane * 16;
  const v2u tagv = {(unsigned)tag, (unsigned)(tag >> 32)};
  volatile LAS unsigned* cw = (volatile LAS unsigned*)(L + L_CNT);
  v4u hf[24];
  const int ho = WS_HT + (4 * rt * 48 + 6 * w) * 1024;
#define HOFF(i) (ho + (((tw + (i) / 6) & 3) * 48 + (i) % 6) * 1024)
  const bool okH = RFL(cw[C_OKH]) != 0;
  {
    const int vo = okH ? l16 : -1;
#pragma unroll
    for (int i = 0; i < 6; ++i) LD128(hf[i], sws, vo, HOFF(i));
  }
  mid();
  STAMP(17);
  if (!okH) {
    if (tw == 0) { if (RFL(cw[C_OKH]) == 0) { spin1(sws, WS_FH + p * FGS + l15 * 8, tag); cw[C_OKH] = 1u; } }
    else lds_wait(L, C_OKH, 1);
    asm volatile("" ::: "memory");
#pragma unroll
    for (int i = 0; i < 6; ++i)
      asm volatile("buffer_load_dwordx4 %0, %1, %2, %3 offen sc1" : "+v"(hf[i]) : "v"(l16), "s"(sws), "s"(HOFF(i)) : "memory");
#pragma unroll
    for (int i = 0; i < NY; ++i) { const int d0 = -1; ST64(tagv, sws, d0, 0); }
  }
  PRIO(0);
  STAMP(19);
  f32x4 a2[2][5];
  const int nfin = (tw + 3) & 3;
  const int trow = 64 * rt + 16 * nfin + 4 * q + 2 * fh;
  v2u qa0, qa1, gg; unsigned qb0, qb1;
  v4u yl0, yl1;
  v2u fyv;
  bool okY = false;
  const int yvo = (tid >> 3) * 4096 + (tid & 7) * 16;
  f32x4 prev[5];
  unsigned fl = 0;
#pragma unroll
  for (int i = 0; i < 24; ++i) {
    const int ph = i / 6, s = i % 6;
    const int no = (tw + ph + 3) & 3;
    LAS unsigned char* xo = L + L_XCH + (fh * 4 + no) * 5120 + l16;
    LAS unsigned char* xc = L + L_XCH + (fh * 4 + ((tw + ph) & 3)) * 5120 + l16;
    WAIT_V(i < 6 ? 5 + NY : i < 18 ? 5 : i < 20 ? 28 - i : 30 - i);
    PIN(hf[i]);
    if (ph == 0) {
#pragma unroll
      for (int m = 0; m < 5; ++m) PIN(wf[s * 5 + m]);
    }
    SCHED();
#pragma unroll
    for (int m = 0; m < 5; ++m) {
      a2[ph & 1][m] = mfma(hf[i], wf[s * 5 + m], s == 0 ? f32x4{0.f, 0.f, 0.f, 0.f} : a2[ph & 1][m]);
      SCHED();
      if (m == 0 && i + 6 < 24) LD128(hf[i + 6], sws, l16, HOFF(i + 6));
      if (ph >= 1 && s >= 1 && s <= 3 && (m == 1 || m == 3)) {
        const int mm = 2 * (s - 1) + (m >> 1);
        if (mm < 5) {
          f32x4 t = a2[(ph - 1) & 1][mm];
          if (ph >= 2) t = add4s(t, prev[mm]);
          *(volatile LAS f32x4*)(xo + mm * 1024) = t;
        } else cw[C_RING + fh * 4 + no] = ph;
      }
      SCHED();
    }
    if (i == 6) {
      if (lane == 0) {
        const unsigned old = __hip_atomic_fetch_add((LAS unsigned*)(L + L_CNT) + C_Y, 1u, __ATOMIC_RELAXED, __HIP_MEMORY_SCOPE_WORKGROUP);
        if (old == 7u) { const int z = 0; ST64(tagv, sws, z, WS_FY + cg * FGS + rt * 8); }
      }
      if ((unsigned)RFL(fl) < 4u) lds_wait(L, fh == 0 ? C_CONV : C_CONVC, 4);
    }
    if (i == 5) fl = cw[fh == 0 ? C_CONV : C_CONVC];
    if (i == 11 || i == 17) fl = cw[C_RING + fh * 4 + ((tw + ph) & 3)];
    if (i == 22) fl = cw[C_RING + fh * 4 + nfin];
    if (i == 12 || i == 18 || i == 23) {
      LAS unsigned char* xr = i < 23 ? xo : xc;
      if ((unsigned)RFL(fl) < (unsigned)(i < 23 ? ph - 1 : 3)) lds_wait(L, C_RING + fh * 4 + (i < 23 ? no : nfin), i < 23 ? ph - 1 : 3);
#pragma unroll
      for (int m = 0; m < 5; ++m) prev[m] = *(volatile LAS f32x4*)(xr + m * 1024);
    }
    if (i == 14 && w == 7) { const int fo = (lane & 31) * 8; LD64(fyv, sws, fo, WS_FY + cg * FGS); }
    if (i == 17) {
      if (w == 7) {
        WAIT_V(3);
        PIN(fyv);
        if (allof(eq1(fyv, tag))) cw[C_OKY] = 1u;
      }
      const int qo = trow * 1280 + l15 * 8, qo2 = trow * 1280 + l15 * 2, go = trow * 4;
      LD64P(qa0, sq, qo, cg * 160);
      LD64P(qa1, sq, qo, cg * 160 + 1280);
      LD16P(qb0, sq, qo2, cg * 160 + 128);
      LD16P(qb1, sq, qo2, cg * 160 + 128 + 1280);
      LD64(gg, sws, go, WS_G);
    }
    if (i == 19) {
      okY = RFL(cw[C_OKY]) != 0;
      const int yo = okY ? yvo : -1, yo1 = (okY && w < 2) ? yvo : -1;
      LD128(yl0, sws, yo, WS_YT + 80 * cg * 4096 + rt * 128);
      LD128(yl1, sws, yo1, WS_YT + (80 * cg + 64) * 4096 + rt * 128);
    }
    SCHED();
  }
  STAMP(20);
  f32x4 z[5];
#pragma unroll
  for (int m = 0; m < 5; ++m) z[m] = a2[1][m] + prev[m];
  LAS unsigned char* fb = L + L_XCH + (fh * 4 + nfin) * 5120 + l16;
  f32x4 s0, s1; f32x2 s2;
  if (fh == 0) { s0 = f32x4{z[0][2], z[1][2], z[2][2], z[3][2]}; s1 = f32x4{z[0][3], z[1][3], z[2][3], z[3][3]}; s2 = f32x2{z[4][2], z[4][3]}; }
  else { s0 = f32x4{z[0][0], z[1][0], z[2][0], z[3][0]}; s1 = f32x4{z[0][1], z[1][1], z[2][1], z[3][1]}; s2 = f32x2{z[4][0], z[4][1]}; }
  *(LAS f32x4*)(fb) = s0; *(LAS f32x4*)(fb + 1024) = s1; *(LAS f32x2*)(fb + 2048) = s2;
  WAIT_V(0);
  PIN(yl0); PIN(yl1);
  if (!okY) {
    if (w == 7) { spin1(sws, WS_FY + cg * FGS + (lane & 31) * 8, tag); cw[C_OKY] = 1u; }
    else lds_wait(L, C_OKY, 1);
    const int yo1 = w < 2 ? yvo : -1;
    LD128(yl0, sws, yvo, WS_YT + 80 * cg * 4096 + rt * 128);
    LD128(yl1, sws, yo1, WS_YT + (80 * cg + 64) * 4096 + rt * 128);
    WAIT_V(0);
    PIN(yl0); PIN(yl1);
  }
  PIN(qa0); PIN(qa1); PIN(qb0); PIN(qb1); PIN(gg);
  { const int ch = tid >> 3; *(LAS v4u*)(L + L_YI + ch * 128 + (((tid & 7) ^ ((ch >> 2) & 7)) * 16)) = yl0; }
  if (w < 2) { const int ch = 64 + (tid >> 3); *(LAS v4u*)(L + L_YI + ch * 128 + (((tid & 7) ^ ((ch >> 2) & 7)) * 16)) = yl1; }
  BAR();
  LAS unsigned char* rb = L + L_XCH + ((fh ^ 1) * 4 + nfin) * 5120 + l16;
  f32x4 r0 = *(LAS f32x4*)(rb), r1 = *(LAS f32x4*)(rb + 1024); f32x2 r2 = *(LAS f32x2*)(rb + 2048);
  if (fh == 0) { r0 += f32x4{z[0][0], z[1][0], z[2][0], z[3][0]}; r1 += f32x4{z[0][1], z[1][1], z[2][1], z[3][1]}; r2 += f32x2{z[4][0], z[4][1]}; }
  else { r0 += f32x4{z[0][2], z[1][2], z[2][2], z[3][2]}; r1 += f32x4{z[0][3], z[1][3], z[2][3], z[3][3]}; r2 += f32x2{z[4][2], z[4][3]}; }
  unsigned yv[5];
  const int chunk = 2 * nfin + (q >> 1), wi = ((q & 1) * 2 + fh) * 4;
#pragma unroll
  for (int m = 0; m < 4; ++m) yv[m] = *(LAS unsigned*)(L + L_YI + (4 * l15 + m) * 128 + ((chunk ^ (l15 & 7)) * 16) + wi);
  yv[4] = *(LAS unsigned*)(L + L_YI + (64 + l15) * 128 + ((chunk ^ ((l15 >> 2) & 7)) * 16) + wi);
  const v2u b2a = *(LAS v2u*)(L + L_B2 + l15 * 8);
  const float b2v[5] = {blo(b2a.x), bhi(b2a.x), blo(b2a.y), bhi(b2a.y), blo((unsigned)*(LAS u16*)(L + L_B2 + 128 + l15 * 2))};
  const float g0 = u2f(gg.x), g1 = u2f(gg.y);
  const float q0[5] = {blo(qa0.x), bhi(qa0.x), blo(qa0.y), bhi(qa0.y), blo(qb0)};
  const float q1[5] = {blo(qa1.x), bhi(qa1.x), blo(qa1.y), bhi(qa1.y), blo(qb1)};
  const float bs0[5] = {r0[0], r0[1], r0[2], r0[3], r2[0]}, bs1[5] = {r1[0], r1[1], r1[2], r1[3], r2[1]};
  float o0v[5], o1v[5];
#pragma unroll
  for (int m = 0; m < 5; ++m) {
    o0v[m] = g0 * (q0[m] * blo(yv[m])) + (1.f - g0) * (bs0[m] + b2v[m]);
    o1v[m] = g1 * (q1[m] * bhi(yv[m])) + (1.f - g1) * (bs1[m] + b2v[m]);
  }
  const v4u so = srd(P.out, S * D * 2);
  const int qo = trow * 1280 + l15 * 8, qo2 = trow * 1280 + l15 * 2;
  const v2u w0 = {pk(o0v[0], o0v[1]), pk(o0v[2], o0v[3])}, w1 = {pk(o1v[0], o1v[1]), pk(o1v[2], o1v[3])};
  const unsigned w2 = pk(o0v[4], 0.f), w3 = pk(o1v[4], 0.f);
  STO64(w0, so, qo, cg * 160);
  STO64(w1, so, qo, cg * 160 + 1280);
  STO16(w2, so, qo2, cg * 160 + 128);
  STO16(w3, so, qo2, cg * 160 + 128 + 1280);
  STAMP(21);
}

template <int PART> DEVI void conv_half(LAS unsigned char* pa, LAS unsigned char* pb, f32x4 (&ah)[4]) {
  constexpr int mx = HalfB<PART>::mx, n = mx + 1;
  v4u B[2][n], A[2][n];
  A[0][0] = *(LAS v4u*)(pa);
#pragma unroll
  for (int d = mx; d >= 0; --d) B[0][d] = *(LAS v4u*)(pb + (7 - d) * 512);
#pragma unroll
  for (int c = 1; c < 4; ++c) A[0][c] = *(LAS v4u*)(pa + c * 512);
#pragma unroll
  for (int g = 0; g < 2 * n; ++g) {
    const int ee = g / n, c = g % n;
    if (ee == 0) B[1][mx - c] = *(LAS v4u*)(pb + 64 + (7 - (mx - c)) * 512);
    if (g + 4 < 2 * n) A[(g + 4) / n][(g + 4) % n] = *(LAS v4u*)(pa + ((g + 4) / n) * 64 + ((g + 4) % n) * 512);
    SCHED();
#pragma unroll
    for (int t = 0; t < 4; ++t)
      if (HalfB<PART>::b[t] >= c) ah[t] = mfma(A[ee][c], B[ee][HalfB<PART>::b[t] - c], g == 0 ? f32x4{0.f, 0.f, 0.f, 0.f} : ah[t]);
    SCHED();
  }
}

template <class Hook> DEVI void conv_full(LAS unsigned char* pa, LAS unsigned char* pb, LAS unsigned char* po, Hook hook) {
  f32x4 af[8];
  v4u B[8], A[4], Bzs;
  A[0] = *(LAS v4u*)(pa);
#pragma unroll
  for (int d = 7; d >= 0; --d) B[d] = *(LAS v4u*)(pb + (7 - d) * 512);
  A[1] = *(LAS v4u*)(pa + 512);
  A[2] = *(LAS v4u*)(pa + 1024);
  SCHED();
  hook(-1, 0);
  SCHED();
#pragma unroll
  for (int g = 0; g < 16; ++g) {
    const int ee = g >> 3, c = g & 7;
    v4u Bn, Bz;
#pragma unroll
    for (int b = 7; b >= c; --b) {
      af[b] = mfma(A[g & 3], B[b - c], g == 0 ? f32x4{0.f, 0.f, 0.f, 0.f} : af[b]);
      SCHED();
      const int k = 7 - b, last = 7 - c;
      if (k == 0 && g + 3 < 16) A[(g + 3) & 3] = *(LAS v4u*)(pa + ((g + 3) >> 3) * 64 + ((g + 3) & 7) * 512);
      if (k == (last < 1 ? last : 1)) {
        if (ee == 0 && c > 0) Bn = *(LAS v4u*)(pb + 64 + (c - 1) * 512);
        if (ee == 1 && c > 0) *(LAS f32x4*)(po + (c - 1) * 1024) = af[c - 1];
      }
      if (k == (last < 2 ? last : 2)) {
        if (ee == 0 && c == 6) Bz = *(LAS v4u*)(pb + 64 + 7 * 512);
        hook(g, 0);
      }
      if (k == 4) hook(g, 1);
      SCHED();
    }
    if (ee == 0 && c > 0) B[8 - c] = Bn;
    if (ee == 0 && c == 6) Bzs = Bz;
    if (ee == 0 && c == 7) B[0] = Bzs;
  }
  *(LAS f32x4*)(po + 7 * 1024) = af[7];
}

DEVI v2u conv_fin(LAS unsigned char* L, LAS unsigned char* src, int st, int cs, int b, float dd, int lane) {
  const f32x4 y = add4s(add4s(*(LAS f32x4*)(src), *(LAS f32x4*)(src + st)), add4s(*(LAS f32x4*)(src + 2 * st), *(LAS f32x4*)(src + 3 * st)));
  const v2u uu = *(LAS v2u*)(L + L_U + cs * UCH + UCP + (2044 - 256 * b - 4 * lane) * 2);
  return v2u{pk(fmas(bhi(uu.y), dd, y[0]), fmas(blo(uu.y), dd, y[1])), pk(fmas(bhi(uu.x), dd, y[2]), fmas(blo(uu.x), dd, y[3]))};
}

__global__ NOPK void __launch_bounds__(512, 2) fused(Params P) {
  __shared__ __attribute__((aligned(16))) unsigned char smem[LDS_BYTES];
  LAS unsigned char* L = (LAS unsigned char*)smem;
  const int tid = threadIdx.x, lane = tid & 63, w = RFL(tid >> 6);
  const int bx = blockIdx.x & 7, bi = blockIdx.x >> 3;
  STAMP(0);
  v4u pfjunk = {0u, 0u, 0u, 0u};
  if (w == 3 || w == 2) {
    u64 pc; unsigned len;
    asm volatile("s_getpc_b64 %0\ngm_pc0:\n\ts_mov_b32 %1, gm_code_end-gm_pc0-16" : "=s"(pc), "=s"(len));
    const unsigned off = min((unsigned)((bi + (w == 2 ? 32 : 0)) * 1024 + lane * 16), len);
    const u64 a = pc + off;
    asm volatile("global_load_dwordx4 %0, %1, off" : "+v"(pfjunk) : "v"(a) : "memory");
  }
  const int p = 4 * (bx >> 1) + (bi >> 3), j = 8 * (bx & 1) + (bi & 7);
  const int rt = 8 * (bx >> 1) + (bi >> 2), cg = 4 * (bx & 1) + (bi & 3);
  const int l15 = lane & 15, q = lane >> 4, l16 = lane * 16;
  const unsigned lbase = (unsigned)(size_t)L;
  const bool isw2 = bi < 12;
  const int aidx = isw2 ? bx * 12 + bi : bx * 20 + (bi - 12);
  const int art = RFL(aidx / 10), act = aidx - art * 10;
  if (isw2) {
    const v4u sw2 = srd(P.w2, F * D * 2);
    const int so = (aidx >> 1) * 32 * 1280 + (aidx & 1) * 640;
#pragma unroll
    for (int i = 0; i < 3; ++i) {
      const int e = (w + 8 * i) * 64 + lane, r = e / 40, c = e - r * 40;
      if (i < 2 || w < 4) GLDS_NT(sw2, lbase + L_W2B + (w + 8 * i) * 1024, r * 1280 + c * 16, so);
    }
  } else {
    const v4u sk_ = srd(P.k, S * D * 2), sv_ = srd(P.v, S * D * 2);
    const int r0 = 16 * w + (lane >> 3);
    const int sw0 = ((r0 >> 1) & 1) * 2;
    const int vo0 = (128 * art + r0) * 1280 + act * 128 + (((lane & 7) ^ sw0) * 16);
    const int vo1 = (128 * art + r0 + 8) * 1280 + act * 128 + (((lane & 7) ^ sw0 ^ 4) * 16);
    GLDS_NT(sk_, lbase + L_KT + w * 2048, vo0 - 4 * 1280, 0);
    GLDS_NT(sk_, lbase + L_KT + w * 2048 + 1024, vo1 - 4 * 1280, 0);
    GLDS_NT(sv_, lbase + L_VT + w * 2048, vo0, 0);
    GLDS_NT(sv_, lbase + L_VT + w * 2048 + 1024, vo1, 0);
    if (w == 0) GLDS_NT(sk_, lbase + L_KT + 16384, vo0 + 124 * 1280, 0);
    if (w == 1) GLDS(srd(P.sk, D * 8), lbase + L_SK, l16, act * 512);
  }
  if (w == 2) {
    GLDS(srd(P.gw, 1280), lbase + L_GW, l16, 0);
    GLDS(srd(P.gw, 1280), lbase + L_GW + 1024, l16, 1024);
  }
  if (w == 3) {
    GLDS(srd(P.b1, F * 2), lbase + L_B1, l16, j * 192);
    if (lane < 10) GLDS(srd(P.b2, D * 2), lbase + L_B2, l16, cg * 160);
  }
  const v4u sws = srd(P.ws, WS_END);
  const v4u sq = srd(P.q, S * D * 2);
  if (tid < 16) *(LAS unsigned*)(L + L_CNT + tid * 4) = 0u;
  int rot_ = (((bi & 7) * 5) >> 2) + (((bi >> 3) * 5) >> 1); if (rot_ >= 10) rot_ -= 10;
  const int rot = RFL(rot_);
#define CC(c) RFL((c) + rot >= 10 ? (c) + rot - 10 : (c) + rot)
  const int tw = w & 3;
  const int w2o = WS_W2T + (cg * 48 + 6 * w) * 5 * 1024;
#define W2LD(i) LD128(wf[i], sws, l16, w2o + (i) * 1024)
  const int chb = 80 * cg + 5 * (rt >> 1), par = rt & 1;
  const int ch0 = chb + 3 * par, chH = chb + 2;
  const int m8 = tid & 255;
  LAS unsigned char* pa = L + L_H + (l15 & 3) * HCP + (8 * (l15 >> 2) + q) * 16 + 128 * tw;
  LAS unsigned char* pb = L + L_U + (l15 & 1) * UCP + (7 - (l15 >> 1) + q) * 16 + 128 * tw;
#define YARRIVE() do { if (lane == 0) { \
      const unsigned old_ = __hip_atomic_fetch_add((LAS unsigned*)(L + L_CNT) + C_Y, 1u, __ATOMIC_RELAXED, __HIP_MEMORY_SCOPE_WORKGROUP); \
      if (old_ == 7u) { const int z_ = 0; ST64(tagv, sws, z_, WS_FY + cg * FGS + rt * 8); } } } while (0)

  if (w < 4) {
    PRIO(2);
    unsigned d1;
    asm volatile("s_load_dword %0, %1, %2" : "=&s"(d1) : "s"(P.ssmD), "s"(((ch0 + 1) * 2) & ~3) : "memory");
    __builtin_amdgcn_s_barrier();
    v2u ep;
    { const int z = 0; LD64(ep, sws, z, WS_EP); }
    WAIT_V(0);
    PIN(ep);
    PIN(pfjunk);
    const u64 tag = (((u64)RFL(ep.y) << 32) | (unsigned)RFL(ep.x)) + 1;
    const v2u tagv = {(unsigned)tag, (unsigned)(tag >> 32)};
    BAR();
    asm volatile("" : "+s"(d1));
    STAMP(1);
    if (!isw2) {
      const v2u skv = *(LAS v2u*)(L + L_SK + (16 * tw + l15) * 8);
      const unsigned sa = __builtin_amdgcn_alignbit(skv.y, skv.y, 16), sb = __builtin_amdgcn_alignbit(skv.x, skv.x, 16);
      LAS unsigned char* ko = L + L_KVO + tw * 4096;
      v2u kr[4][3], vr[4][2];
#pragma unroll
      for (int th = 0; th < 4; ++th) {
        const int r = 32 * th + 8 * q + (l15 >> 2);
        const int b1_ = (r >> 1) & 1, b3 = (r >> 3) & 1;
        const int ua = (tw ^ b1_ ^ (b3 << 1)) * 32 + (lane & 3) * 8, ub = (tw ^ b1_ ^ ((b3 ^ 1) << 1)) * 32 + (lane & 3) * 8;
        kr[th][0] = trd(L + L_KT + r * 128 + ua); kr[th][1] = trd(L + L_KT + (r + 4) * 128 + ua); kr[th][2] = trd(L + L_KT + (r + 8) * 128 + ub);
        vr[th][0] = trd(L + L_VT + r * 128 + ua); vr[th][1] = trd(L + L_VT + (r + 4) * 128 + ua);
      }
#pragma unroll
      for (int th = 0; th < 4; ++th) {
        const unsigned e[6] = {kr[th][0].x, kr[th][0].y, kr[th][1].x, kr[th][1].y, kr[th][2].x, kr[th][2].y};
        unsigned od[5];
#pragma unroll
        for (int a = 0; a < 5; ++a) od[a] = __builtin_amdgcn_alignbit(e[a + 1], e[a], 16);
        const unsigned vw[4] = {vr[th][0].x, vr[th][0].y, vr[th][1].x, vr[th][1].y};
        float o[8];
#pragma unroll
        for (int a = 0; a < 4; ++a) {
          o[2 * a] = dot2(od[a + 1], sb, dot2(od[a], sa, 0.f)) * blo(vw[a]);
          o[2 * a + 1] = dot2(e[a + 2], sb, dot2(e[a + 1], sa, 0.f)) * bhi(vw[a]);
        }
        const v4u ov = {pk(o[7], o[6]), pk(o[5], o[4]), pk(o[3], o[2]), pk(o[1], o[0])};
        const int cidx = 15 - (4 * th + q);
        *(LAS v4u*)(ko + l15 * 256 + ((cidx ^ (l15 & 7)) * 16)) = ov;
      }
      WAIT_L0();
      v4u ow[4];
#pragma unroll
      for (int ps = 0; ps < 4; ++ps) { const int ch = 4 * ps + q; ow[ps] = *(LAS v4u*)(ko + ch * 256 + ((l15 ^ (ch & 7)) * 16)); }
      const int kvo = q * 4096 + l15 * 16;
#pragma unroll
      for (int ps = 0; ps < 4; ++ps) ST128(ow[ps], sws, kvo, WS_KVT + (64 * act + 16 * tw + 4 * ps) * 4096 + (1920 - 128 * art) * 2);
    } else {
      const int so = WS_W2T + (((aidx & 1) * 4 + tw) * 48 + (aidx >> 1)) * 5 * 1024;
      v2u r[8];
#pragma unroll
      for (int jj = 0; jj < 8; ++jj) r[jj] = *(LAS v2u*)(L + L_W2B + (8 * q + jj) * 640 + tw * 160 + l15 * 8);
      const int ro = (8 * q + (l15 >> 2)) * 640 + tw * 160 + 128 + (lane & 3) * 8;
      const v4u f4 = cat(trd(L + L_W2B + ro), trd(L + L_W2B + ro + 4 * 640));
      v4u f0, f1, f2, f3;
#pragma unroll
      for (int a = 0; a < 4; ++a) {
        f0[a] = __builtin_amdgcn_perm(r[2 * a + 1].x, r[2 * a].x, 0x05040100u);
        f1[a] = __builtin_amdgcn_perm(r[2 * a + 1].x, r[2 * a].x, 0x07060302u);
        f2[a] = __builtin_amdgcn_perm(r[2 * a + 1].y, r[2 * a].y, 0x05040100u);
        f3[a] = __builtin_amdgcn_perm(r[2 * a + 1].y, r[2 * a].y, 0x07060302u);
      }
      ST128(f0, sws, l16, so);
      ST128(f1, sws, l16, so + 1024);
      ST128(f2, sws, l16, so + 2048);
      ST128(f3, sws, l16, so + 3072);
      ST128(f4, sws, l16, so + 4096);
    }
    STAMP(2);
    v4u wf[30];
    f32x4 acc[6][2], accg = {0.f, 0.f, 0.f, 0.f};
#pragma unroll
    for (int m = 0; m < 6; ++m) {
      const v2u bb = *(LAS v2u*)(L + L_B1 + (16 * m + 4 * q) * 2);
      const f32x4 bv = {blo(bb.x), bhi(bb.x), blo(bb.y), bhi(bb.y)};
      acc[m][0] = bv; acc[m][1] = bv;
    }
    const int nsw = j & 1;
    const bool gatew = (j >> 1) == tw;
    {
      int xa[2][2], wa[2];
#pragma unroll
      for (int n = 0; n < 2; ++n)
#pragma unroll
        for (int ks = 0; ks < 2; ++ks) xa[n][ks] = (32 * tw + 16 * (n ^ nsw) + l15) * 128 + (((4 * ks + q) ^ (l15 >> 1)) * 16);
#pragma unroll
      for (int mp = 0; mp < 2; ++mp) wa[mp] = 16384 + (8 * q + (l15 >> 2)) * 192 + ((mp ^ (q & 1)) * 32) + (lane & 3) * 8;
      v4u fb[2][2]; v2u fa[2][6][2];
#define RDA(ks, m, hh) fa[ks][m][hh] = trd(sb_ + wa[(m) & 1] + ((m) >> 1) * 64 + (ks) * 6144 + (hh) * 768)
#define M1(ks, m, n) acc[m][n] = mfma(cat(fa[ks][m][0], fa[ks][m][1]), fb[ks][n], acc[m][n])
#define MM(ks, m) do { M1(ks, m, 0); M1(ks, m, 1); } while (0)
#define STEP(c, ks, DO) do { LAS unsigned char* sb_ = L + ((c) % 3) * STG; \
        fb[ks][0] = *(LAS v4u*)(sb_ + xa[0][ks]); RDA(ks, 0, 0); SCHED(); \
        _Pragma("unroll") for (int i_ = 0; i_ < 12; ++i_) { \
          if (DO) M1((ks) ^ 1, i_ >> 1, i_ & 1); \
          SCHED(); \
          if (i_ == 0) RDA(ks, 0, 1); else if (i_ == 1) fb[ks][1] = *(LAS v4u*)(sb_ + xa[1][ks]); else RDA(ks, i_ >> 1, i_ & 1); \
          SCHED(); } } while (0)
#pragma unroll
      for (int c = 0; c < 10; ++c) {
        if (c == 2) WAIT_V(0);
        BAR();
        STAMP(3 + c);
        if (c == 2 && tid == 0) { const int z = 0; ST64(tagv, sws, z, WS_FA + blockIdx.x * 8); }
        STEP(c, 0, c > 0);
        if (c >= 8) { W2LD(2 * (c - 8)); SCHED(); }
        STEP(c, 1, true);
        if (c >= 8) { W2LD(2 * (c - 8) + 1); SCHED(); }
        if (gatew) {
          const v4u g0 = *(LAS v4u*)(L + L_GW + CC(c) * 128 + q * 16), g1 = *(LAS v4u*)(L + L_GW + CC(c) * 128 + 64 + q * 16);
          accg = mfma(g0, fb[0][0], accg);
          accg = mfma(g1, fb[1][0], accg);
        }
        SCHED();
      }
      MM(1, 0); MM(1, 1); MM(1, 2); MM(1, 3); MM(1, 4); MM(1, 5);
    }
    SCHED();
    STAMP(13);
    if (gatew) {
      const float z = rbf(accg[0]);
      const float g = rbf(__builtin_amdgcn_rcpf(1.f + __builtin_amdgcn_exp2f(-1.4426950408889634f * z)));
      const int go = lane < 16 ? lane * 4 : -1;
      const unsigned gu = f2u(g);
      ST32(gu, sws, go, WS_G + (128 * p + 32 * tw + 16 * nsw) * 4);
    }
    const int hvo = (((q & 1) ^ nsw) * 48) * 1024 + (q >> 1) * 256 + l15 * 16;
#pragma unroll
    for (int m = 0; m < 6; ++m) {
      unsigned pkd[2][2];
      gelu8(acc[m][0], acc[m][1], pkd[0][0], pkd[0][1], pkd[1][0], pkd[1][1]);
      const auto s0 = __builtin_amdgcn_permlane16_swap(pkd[0][0], pkd[1][0], false, false);
      const auto s1 = __builtin_amdgcn_permlane16_swap(pkd[0][1], pkd[1][1], false, false);
      const v4u hv = {s0[0], s1[0], s0[1], s1[1]};
      ST128(hv, sws, hvo, WS_HT + ((8 * p + 2 * tw) * 48 + 3 * j + (m >> 1)) * 1024 + (m & 1) * 512);
      if (m < 5) W2LD(4 + m);
      if (m == GM_STGM) lds_arrive(L, C_STG, lane);
      SCHED();
    }
    STAMP(14);
    PRIO(1);
    lds_wait(L, C_STG, 8);
    v2u fhv;
    conv_full(pa + HCH, pb + UCH, L + L_R1 + tw * 8192 + l16, [&](int g, int k) NOPK __attribute__((always_inline)) {
      if (g < 0) W2LD(9);
      else if ((g & 7) < 4) W2LD(10 + (g >> 3) * 10 + 2 * (g & 7) + k);
      else if ((g & 7) < 6) W2LD(10 + (g >> 3) * 10 + 4 + (g & 7));
      if (g == GM_HPUB && k == 0) {
        WAIT_V(GM_HPUB < 4 ? 2 + 2 * GM_HPUB : GM_HPUB < 6 ? 6 + GM_HPUB : 11);
        if (lane == 0) {
          const unsigned old = __hip_atomic_fetch_add((LAS unsigned*)(L + L_CNT) + C_H, 1u, __ATOMIC_RELAXED, __HIP_MEMORY_SCOPE_WORKGROUP);
          if (old == 3u) { const int z = 0; ST64(tagv, sws, z, WS_FH + p * FGS + j * 8); }
        }
        STAMP(15);
      }
      if (g == 11 && k == 1 && w == 0) { const int fo = l15 * 8; LD64(fhv, sws, fo, WS_FH + p * FGS); }
    });
    if (w == 0) {
      WAIT_V(2);
      PIN(fhv);
      if (allof(eq1(fhv, tag))) *((volatile LAS unsigned*)(L + L_CNT) + C_OKH) = 1u;
    }
    lds_arrive(L, C_CONVC, lane);
    STAMP(16);
    lds_wait(L, C_CONVC, 4);
    gemm2<2>(P, L, wf, sws, sq, tag, tid, lane, w, p, rt, cg, [&]() NOPK __attribute__((always_inline)) {
      const float dd1 = u2f(((ch0 + 1) & 1) ? (d1 & 0xffff0000u) : (d1 << 16));
      const v2u y0 = conv_fin(L, L + L_R1 + (2 * tw) * 1024 + l16, 8192, 1, 2 * tw, dd1, lane);
      const v2u y1 = conv_fin(L, L + L_R1 + (2 * tw + 1) * 1024 + l16, 8192, 1, 2 * tw + 1, dd1, lane);
      const int yo = lane * 8;
      ST64(y0, sws, yo, WS_YT + (ch0 + 1) * 4096 + 1024 * tw);
      ST64(y1, sws, yo, WS_YT + (ch0 + 1) * 4096 + 1024 * tw + 512);
    } SPASS);
  } else {
    unsigned d0, dH;
    asm volatile("s_load_dword %0, %2, %3\n\ts_load_dword %1, %2, %4" : "=&s"(d0), "=&s"(dH) : "s"(P.ssmD), "s"((ch0 * 2) & ~3), "s"((chH * 2) & ~3) : "memory");
    __builtin_amdgcn_s_barrier();
    v2u ep;
    { const int z = 0; LD64(ep, sws, z, WS_EP); }
    v4u hw[3]; v2u hp[3];
    {
      const v4u sh = srd(P.ssmk, D * S * 2);
      const int o0 = m8 * 16, o1 = m8 == 0 ? -1 : m8 * 16 - 8;
      LD128NT(hw[0], sh, o0, ch0 * 4096); LD64P(hp[0], sh, o1, ch0 * 4096);
      LD128NT(hw[1], sh, o0, (ch0 + 1) * 4096); LD64P(hp[1], sh, o1, (ch0 + 1) * 4096);
      LD128P(hw[2], sh, o0, chH * 4096); LD64P(hp[2], sh, o1, chH * 4096);
    }
    const v4u sw1 = srd(P.w1, D * F * 2);
    int xvo[2], wvo[3];
#pragma unroll
    for (int i = 0; i < 2; ++i) { const int r = 32 * tw + 8 * i + (lane >> 3); xvo[i] = (128 * p + r) * 1280 + (((lane & 7) ^ ((r >> 1) & 7)) * 16); }
#pragma unroll
    for (int i = 0; i < 3; ++i) { const int e = i * 64 + lane, kr = e / 12, ps = e - kr * 12; wvo[i] = (16 * tw + kr) * 3072 + j * 192 + ((ps ^ (2 * ((kr >> 3) & 1))) * 16); }
#define SLOAD(c) do { const unsigned sb_ = lbase + ((c) % 3) * STG; const int cc_ = CC(c); \
      GLDS(sq, sb_ + tw * 4096, xvo[0], cc_ * 128); GLDS(sw1, sb_ + 16384 + tw * 3072, wvo[0], cc_ * 196608); \
      GLDS(sq, sb_ + tw * 4096 + 1024, xvo[1], cc_ * 128); GLDS(sw1, sb_ + 16384 + tw * 3072 + 1024, wvo[1], cc_ * 196608); \
      GLDS(sq, sb_ + tw * 4096 + 2048, xvo[0], cc_ * 128 + 16 * 1280); GLDS(sw1, sb_ + 16384 + tw * 3072 + 2048, wvo[2], cc_ * 196608); \
      GLDS(sq, sb_ + tw * 4096 + 3072, xvo[1], cc_ * 128 + 16 * 1280); } while (0)
    SLOAD(0);
    WAIT_V(13);
    PIN(ep);
    const u64 tag = (((u64)RFL(ep.y) << 32) | (unsigned)RFL(ep.x)) + 1;
    const v2u tagv = {(unsigned)tag, (unsigned)(tag >> 32)};
    BAR();
    asm volatile("" : "+s"(d0), "+s"(dH));
    STAMP(1);
    v4u wf[30];
    v4u uw[3], fa0, fa1;
    SLOAD(1);
    SLOAD(2);
#pragma unroll
    for (int c = 0; c < 10; ++c) {
      if (c == 0) WAIT_V(14); else if (c < 9) WAIT_V(7); else WAIT_V(3);
      if (c == 8 && w == 4) {
        PIN(fa0); PIN(fa1);
        if (!allof(eq2(fa0, tag) && eq2(fa1, tag))) spin4(sws, WS_FA + l16, tag);
      }
      __builtin_amdgcn_s_barrier();
      asm volatile("" ::: "memory");
      STAMP(3 + c);
      if (c == 7 && w == 4) { LD128(fa0, sws, l16, WS_FA); LD128(fa1, sws, l16, WS_FA + 1024); }
      if (c > 0 && c + 2 < 10) SLOAD(c + 2);
      if (c == 8) {
        if (blockIdx.x == 0 && tid == 256) { const int z = 0; ST64(tagv, sws, z, WS_EP); }
        const int o0 = m8 * 16;
        LD128(uw[0], sws, o0, WS_KVT + ch0 * 4096);
        LD128(uw[1], sws, o0, WS_KVT + (ch0 + 1) * 4096);
        LD128(uw[2], sws, o0, WS_KVT + chH * 4096);
      }
    }
    PRIO(1);
    W2LD(0); W2LD(1);
    *(LAS v4u*)(L + L_H + (m8 / 24) * HCP + (m8 % 24) * 16) = v4u{0u, 0u, 0u, 0u};
    if (m8 < 32) *(LAS v4u*)(L + L_H + ((m8 + 256) / 24) * HCP + ((m8 + 256) % 24) * 16) = v4u{0u, 0u, 0u, 0u};
    else if (m8 < 128) { const int e = m8 - 32, cp = e >> 4, i = e & 15; if (i < 15 && (i > 0 || !(cp & 1))) *(LAS v2u*)(L + L_U + cp * UCP + 4088 + i * 8) = v2u{0u, 0u}; }
#pragma unroll
    for (int cs = 0; cs < 3; ++cs) {
      PIN(hw[cs]); PIN(hp[cs]);
      const v4u wv = hw[cs]; const v2u pv = hp[cs];
      LAS unsigned char* a = L + L_H + cs * HCH + (m8 + 24) * 16;
      const unsigned a0 = __builtin_amdgcn_alignbit(pv.y, pv.x, 16), a1 = __builtin_amdgcn_alignbit(wv.x, pv.y, 16),
                     a2 = __builtin_amdgcn_alignbit(wv.y, wv.x, 16), a3 = __builtin_amdgcn_alignbit(wv.z, wv.y, 16),
                     a4 = __builtin_amdgcn_alignbit(wv.w, wv.z, 16);
      *(LAS v4u*)(a) = v4u{a0, a1, a2, a3};
      *(LAS v4u*)(a + HCP) = v4u{pv.y, wv.x, wv.y, wv.z};
      W2LD(2 + 2 * cs);
      *(LAS v4u*)(a + 2 * HCP) = v4u{a1, a2, a3, a4};
      *(LAS v4u*)(a + 3 * HCP) = wv;
      W2LD(3 + 2 * cs);
    }
    WAIT_V(8);
#pragma unroll
    for (int cs = 0; cs < 3; ++cs) {
      PIN(uw[cs]);
      LAS unsigned char* a = L + L_U + cs * UCH + m8 * 16;
      *(LAS v2u*)(a - 8) = v2u{uw[cs].x, uw[cs].y};
      *(LAS v2u*)(a) = v2u{uw[cs].z, uw[cs].w};
      *(LAS v4u*)(a + UCP) = uw[cs];
      W2LD(8 + cs);
    }
    lds_arrive(L, C_STG, lane);
    STAMP(13);
#pragma unroll
    for (int i = 11; i < 17; ++i) { WAIT_V(3); W2LD(i); }
    lds_wait(L, C_STG, 8);
    STAMP(14);
    v2u fhv;
    {
      f32x4 ah[4];
      if (par == 0) conv_half<0>(pa + 2 * HCH, pb + 2 * UCH, ah); else conv_half<1>(pa + 2 * HCH, pb + 2 * UCH, ah);
      STAMP(15);
      conv_full(pa, pb, L + L_R0 + tw * 8192 + l16, [&](int g, int k) NOPK __attribute__((always_inline)) {
        if (g < 0) {
#pragma unroll
          for (int t = 0; t < 4; ++t) *(LAS f32x4*)(L + L_R1 + 32768 + (tw * 4 + t) * 1024 + l16) = ah[t];
          W2LD(17);
        } else if (g < 4) W2LD(18 + 2 * g + k);
        else if (g < 6) W2LD(22 + g);
        else if (g == 8) W2LD(28 + k);
        if (g == 11 && k == 1 && w == 4) { const int fo = l15 * 8; LD64(fhv, sws, fo, WS_FH + p * FGS); }
      });
    }
    if (w == 4) {
      WAIT_V(0);
      PIN(fhv);
      if (allof(eq1(fhv, tag))) *((volatile LAS unsigned*)(L + L_CNT) + C_OKH) = 1u;
    }
    lds_arrive(L, C_CONV, lane);
    STAMP(16);
    lds_wait(L, C_CONV, 4);
    gemm2<3>(P, L, wf, sws, sq, tag, tid, lane, w, p, rt, cg, [&]() NOPK __attribute__((always_inline)) {
      const float dd0 = u2f((ch0 & 1) ? (d0 & 0xffff0000u) : (d0 << 16)), ddH = u2f((chH & 1) ? (dH & 0xffff0000u) : (dH << 16));
      const int bh = par == 0 ? (tw < 2 ? 7 - tw : 3 - tw) : 5 - tw;
      const v2u y0 = conv_fin(L, L + L_R0 + (2 * tw) * 1024 + l16, 8192, 0, 2 * tw, dd0, lane);
      const v2u y1 = conv_fin(L, L + L_R0 + (2 * tw + 1) * 1024 + l16, 8192, 0, 2 * tw + 1, dd0, lane);
      const v2u y2 = conv_fin(L, L + L_R1 + 32768 + tw * 1024 + l16, 4096, 2, bh, ddH, lane);
      const int yo = lane * 8;
      ST64(y0, sws, yo, WS_YT + ch0 * 4096 + 1024 * tw);
      ST64(y1, sws, yo, WS_YT + ch0 * 4096 + 1024 * tw + 512);
      ST64(y2, sws, yo, WS_YT + chH * 4096 + 512 * bh);
    } SPASS);
  }
  asm volatile("gm_code_end:");
}
}

extern "C" void kernel_launch(void* const* d_in, const int* in_sizes, int n_in, void* d_out, int out_size, void* d_ws, size_t ws_size,
                              hipStream_t stream) {
  using namespace gm;
  Params P;
  P.q = (const u16*)d_in[0]; P.k = (const u16*)d_in[1]; P.v = (const u16*)d_in[2]; P.sk = (const u16*)d_in[3];
  P.ssmk = (const u16*)d_in[4]; P.ssmD = (const u16*)d_in[5]; P.w1 = (const u16*)d_in[7]; P.b1 = (const u16*)d_in[8];
  P.w2 = (const u16*)d_in[9]; P.b2 = (const u16*)d_in[10]; P.gw = (const u16*)d_in[11];
  P.out = (u16*)d_out; P.ws = (unsigned char*)d_ws;
  fused<<<256, 512, 0, stream>>>(P);
}
```
